```python
import torch

def _softmax(x, axis=-1):
    return torch.softmax(x, dim=axis)
def _gelu(x):
    return torch.nn.functional.gelu(x, approximate="tanh")
def _silu(x):
    return torch.nn.functional.silu(x)
def _sigmoid(x):
    return torch.sigmoid(x)
def _layernorm(x, axis=-1, eps=1e-5):
    m = x.mean(dim=axis, keepdim=True)
    v = x.var(dim=axis, keepdim=True, unbiased=False)
    return (x - m) / torch.sqrt(v + eps)
def _scatter_add(tgt, idx, src, axis=0):
    return tgt.clone().index_add_(axis, idx, src)
def _segment_sum(src, seg, n):
    out = torch.zeros((n,) + src.shape[1:], dtype=src.dtype)
    return out.index_add_(0, seg, src)
def _pad(x, pw):
    flat = tuple(v for pair in reversed(pw) for v in pair)
    return torch.nn.functional.pad(x, flat)

kernel_name = 'gated_mix__linear_attn_block__dense_activation_relu__prompt'

def setup_inputs():

    _w = (lambda a: (lambda t: t.to(torch.bfloat16) if t.is_floating_point() else t)(torch.as_tensor(a)))
    def _A_setup():
        B, S, D = 1, 2048, 576
        return {
            "x": _w((lambda *s: torch.randn(*s, dtype=torch.bfloat16) * 0.05)(B, S, D)),
            "wqkv": _w((lambda *s: torch.randn(*s, dtype=torch.bfloat16) * 0.8)(D, 3 * D)),
            "wo": _w((lambda *s: torch.randn(*s, dtype=torch.bfloat16) * 0.2)(D, D)),
        }
    _A_in = _A_setup()

    def _B_setup():
        B, S, D, Dff = 1, 2048, 576, 4608
        return {
            "x": _w((lambda *s: torch.randn(*s, dtype=torch.bfloat16) * 0.05)(B, S, D)),
            "w1": _w((lambda *s: torch.randn(*s, dtype=torch.bfloat16) * 0.05)(D, Dff)),
            "w2": _w((lambda *s: torch.randn(*s, dtype=torch.bfloat16) * 0.05)(Dff, D)),
        }
    _B_in = _B_setup()

    return {
        "A_x": _A_in["x"],
        "A_wqkv": _A_in["wqkv"],
        "A_wo": _A_in["wo"],
        "B_x": _B_in["x"],
        "B_w1": _B_in["w1"],
        "B_w2": _B_in["w2"],
        "gate_w": _w((lambda *s: torch.randn(*s, dtype=torch.bfloat16) * 0.05)(576) * 16.0),
    }

def reference(A_x, A_wqkv, A_wo, B_x, B_w1, B_w2, gate_w):
    def _A_ref(x, wqkv, wo):
        B, S, D = x.shape
        H = 32; Dh = D // H
        qkv = (lambda x, *d: x.permute(*d))((x @ wqkv).reshape(B, S, 3, H, Dh), 2, 0, 3, 1, 4)
        q, k, v = qkv[0], qkv[1], qkv[2]
        phi = lambda t: torch.where(t > 0, t + 1, torch.exp(t)) + 1e-6
        pq, pk = phi(q), phi(k)
        kv = torch.einsum("bhsd,bhse->bhde", pk, v)
        z = torch.einsum("bhsd->bhd", pk)
        num = torch.einsum("bhqd,bhde->bhqe", pq, kv)
        den = torch.einsum("bhqd,bhd->bhq", pq, z)[..., None] + 1e-6
        o = num / den
        return (lambda x, *d: x.permute(*d))(o, 0, 2, 1, 3).reshape(B, S, D) @ wo

    def _B_ref(x, w1, w2):
        h = torch.einsum("bsd,df->bsf", x, w1)
        zero = torch.zeros_like(h)
        h = torch.maximum(h, zero)
        return torch.einsum("bsf,fd->bsd", h, w2)

    x = A_x
    g = _sigmoid((x * gate_w).sum(-1, keepdims=True))
    a = _A_ref(x, A_wqkv, A_wo)
    b = _B_ref(x, B_w1, B_w2)
    return g * a + (1 - g) * b
```

```cpp
#include <hip/hip_runtime.h>
#include <hip/hip_bf16.h>
#include <stdint.h>

typedef __attribute__((ext_vector_type(8))) short bf16x8;
typedef __attribute__((ext_vector_type(4))) short s16x4;
typedef __attribute__((ext_vector_type(4))) float f32x4;
typedef __attribute__((address_space(3))) s16x4* lds_s4p;
typedef unsigned short u16;

#define S_ 2048
#define D_ 576
#define DFF_ 4608
#define H_ 32
#define DH_ 18

__device__ __forceinline__ float bf2f(u16 v) { return __uint_as_float(((unsigned)v) << 16); }
__device__ __forceinline__ u16 f2bf(float f) {
  __hip_bfloat16 b = __float2bfloat16(f);
  return *reinterpret_cast<u16*>(&b);
}
__device__ __forceinline__ float rbf(float f) { return bf2f(f2bf(f)); }

constexpr int BM = 128, BN = 64, BK = 64;
constexpr int A_STRIDE = 144;
constexpr int A_BYTES = BM * A_STRIDE;
constexpr int B_BYTES = 4 * 64 * 32;

__device__ __forceinline__ void gemm_kloop(const u16* __restrict__ A, int lda, const u16* __restrict__ B, int ldb,
                                           int K, int brow, int bcol, char* smem, f32x4 (&acc)[4][2]) {
  const int tid = threadIdx.x, lane = tid & 63, wid = tid >> 6, wr = wid >> 1, wc = wid & 1;
  char* sA = smem;
  char* sB = smem + A_BYTES;
  for (int k0 = 0; k0 < K; k0 += BK) {
    __syncthreads();
    for (int i = 0; i < 4; ++i) {
      int c = tid + 256 * i, r = c >> 3, ch = c & 7;
      uint4 v = *reinterpret_cast<const uint4*>(A + (size_t)(brow + r) * lda + k0 + ch * 8);
      *reinterpret_cast<uint4*>(sA + r * A_STRIDE + ch * 16) = v;
    }
    for (int i = 0; i < 2; ++i) {
      int c = tid + 256 * i, k = c >> 3, ch = c & 7;
      uint4 v = *reinterpret_cast<const uint4*>(B + (size_t)(k0 + k) * ldb + bcol + ch * 8);
      int kpos = k ^ (((k >> 3) & 1) << 2);
      *reinterpret_cast<uint4*>(sB + (ch >> 1) * 2048 + kpos * 32 + (ch & 1) * 16) = v;
    }
    __syncthreads();
    for (int ks = 0; ks < 2; ++ks) {
      bf16x8 af[4], bfr[2];
      for (int m = 0; m < 4; ++m)
        af[m] = *reinterpret_cast<const bf16x8*>(sA + (wr * 64 + m * 16 + (lane & 15)) * A_STRIDE + ks * 64 + (lane >> 4) * 16);
      for (int n = 0; n < 2; ++n) {
        int c = wc * 2 + n;
        s16x4 t[2];
        for (int tt = 0; tt < 2; ++tt) {
          int k = 32 * ks + 8 * (lane >> 4) + 4 * tt + ((lane & 15) >> 2);
          int kpos = k ^ (((k >> 3) & 1) << 2);
          t[tt] = __builtin_amdgcn_ds_read_tr16_b64_v4i16((lds_s4p)(sB + c * 2048 + kpos * 32 + (lane & 3) * 8));
        }
        bfr[n] = (bf16x8){t[0][0], t[0][1], t[0][2], t[0][3], t[1][0], t[1][1], t[1][2], t[1][3]};
      }
      for (int m = 0; m < 4; ++m)
        for (int n = 0; n < 2; ++n)
          acc[m][n] = __builtin_amdgcn_mfma_f32_16x16x32_bf16(af[m], bfr[n], acc[m][n], 0, 0, 0);
    }
  }
}

__device__ __forceinline__ float phi_f(float t) {
  float r = t > 0.f ? rbf(t + 1.f) : rbf(__expf(t));
  return rbf(r + 1e-6f);
}

__global__ __launch_bounds__(256) void k1_gemm(const u16* __restrict__ x, const u16* __restrict__ wqkv,
                                               const u16* __restrict__ w1, u16* __restrict__ pq, u16* __restrict__ pk,
                                               u16* __restrict__ vv, u16* __restrict__ hh) {
  __shared__ __attribute__((aligned(16))) char smem[A_BYTES + B_BYTES];
  int bn = blockIdx.x % 99, bm = blockIdx.x / 99;
  f32x4 acc[4][2] = {};
  const int lane = threadIdx.x & 63, wid = threadIdx.x >> 6, wr = wid >> 1, wc = wid & 1;
  int brow = bm * BM;
  if (bn < 27) {
    gemm_kloop(x, D_, wqkv, 3 * D_, D_, brow, bn * BN, smem, acc);
    for (int m = 0; m < 4; ++m) for (int n = 0; n < 2; ++n) for (int j = 0; j < 4; ++j) {
      int r = brow + wr * 64 + m * 16 + (lane >> 4) * 4 + j;
      int c = bn * BN + wc * 32 + n * 16 + (lane & 15);
      float t = rbf(acc[m][n][j]);
      if (c < D_) pq[(size_t)r * D_ + c] = f2bf(phi_f(t));
      else if (c < 2 * D_) pk[(size_t)r * D_ + c - D_] = f2bf(phi_f(t));
      else vv[(size_t)r * D_ + c - 2 * D_] = f2bf(t);
    }
  } else {
    int bc = (bn - 27) * BN;
    gemm_kloop(x, D_, w1, DFF_, D_, brow, bc, smem, acc);
    for (int m = 0; m < 4; ++m) for (int n = 0; n < 2; ++n) for (int j = 0; j < 4; ++j) {
      int r = brow + wr * 64 + m * 16 + (lane >> 4) * 4 + j;
      int c = bc + wc * 32 + n * 16 + (lane & 15);
      hh[(size_t)r * DFF_ + c] = f2bf(fmaxf(acc[m][n][j], 0.f));
    }
  }
}

__global__ __launch_bounds__(384) void k2_kv(const u16* __restrict__ pk, const u16* __restrict__ vv, float* __restrict__ kvz) {
  __shared__ float spk[256 * DH_], sv[256 * DH_];
  int h = blockIdx.x, s0 = blockIdx.y * 256;
  for (int i = threadIdx.x; i < 256 * DH_; i += 384) {
    int s = i / DH_, d = i % DH_;
    spk[i] = bf2f(pk[(size_t)(s0 + s) * D_ + h * DH_ + d]);
    sv[i] = bf2f(vv[(size_t)(s0 + s) * D_ + h * DH_ + d]);
  }
  __syncthreads();
  int t = threadIdx.x;
  if (t < 342) {
    float a = 0.f;
    if (t < 324) { int d = t / DH_, e = t % DH_; for (int s = 0; s < 256; ++s) a += spk[s * DH_ + d] * sv[s * DH_ + e]; }
    else { int d = t - 324; for (int s = 0; s < 256; ++s) a += spk[s * DH_ + d]; }
    atomicAdd(&kvz[h * 342 + t], a);
  }
}

__global__ __launch_bounds__(256) void k2b_o(const u16* __restrict__ pq, const float* __restrict__ kvz, u16* __restrict__ o) {
  int idx = blockIdx.x * 256 + threadIdx.x;
  int s = idx / D_, c = idx % D_, h = c / DH_, e = c % DH_;
  float num = 0.f, den = 0.f;
  for (int d = 0; d < DH_; ++d) {
    float q = bf2f(pq[(size_t)s * D_ + h * DH_ + d]);
    num += q * rbf(kvz[h * 342 + d * DH_ + e]);
    den += q * rbf(kvz[h * 342 + 324 + d]);
  }
  num = rbf(num); den = rbf(rbf(den) + 1e-6f);
  o[idx] = f2bf(num / den);
}

__global__ __launch_bounds__(256) void k3_out(const u16* __restrict__ x, const u16* __restrict__ gw, const u16* __restrict__ o,
                                              const u16* __restrict__ wo, const u16* __restrict__ hh, const u16* __restrict__ w2,
                                              u16* __restrict__ out) {
  __shared__ __attribute__((aligned(16))) char smem[A_BYTES + B_BYTES];
  __shared__ float sg[BM];
  int bn = blockIdx.x % 9, bm = blockIdx.x / 9, brow = bm * BM, bcol = bn * BN;
  const int lane = threadIdx.x & 63, wid = threadIdx.x >> 6, wr = wid >> 1, wc = wid & 1;
  if (threadIdx.x < BM) {
    float s = 0.f;
    for (int d = 0; d < D_; ++d) s += rbf(bf2f(x[(size_t)(brow + threadIdx.x) * D_ + d]) * bf2f(gw[d]));
    s = rbf(s);
    sg[threadIdx.x] = rbf(1.f / (1.f + __expf(-s)));
  }
  f32x4 acca[4][2] = {}, accb[4][2] = {};
  gemm_kloop(o, D_, wo, D_, D_, brow, bcol, smem, acca);
  gemm_kloop(hh, DFF_, w2, D_, DFF_, brow, bcol, smem, accb);
  for (int m = 0; m < 4; ++m) for (int n = 0; n < 2; ++n) for (int j = 0; j < 4; ++j) {
    int rl = wr * 64 + m * 16 + (lane >> 4) * 4 + j;
    int c = bcol + wc * 32 + n * 16 + (lane & 15);
    float g = sg[rl], a = rbf(acca[m][n][j]), b = rbf(accb[m][n][j]);
    float r = rbf(g * a) + rbf(rbf(1.f - g) * b);
    out[(size_t)(brow + rl) * D_ + c] = f2bf(r);
  }
}

extern "C" void kernel_launch(void* const* d_in, const int* in_sizes, int n_in, void* d_out, int out_size, void* d_ws,
                              size_t ws_size, hipStream_t stream) {
  const u16* x = (const u16*)d_in[0];
  const u16* wqkv = (const u16*)d_in[1];
  const u16* wo = (const u16*)d_in[2];
  const u16* w1 = (const u16*)d_in[4];
  const u16* w2 = (const u16*)d_in[5];
  const u16* gw = (const u16*)d_in[6];
  char* ws = (char*)d_ws;
  float* kvz = (float*)ws;
  u16* pq = (u16*)(ws + 65536);
  u16* pk = pq + S_ * D_;
  u16* vv = pk + S_ * D_;
  u16* o = vv + S_ * D_;
  u16* hh = o + S_ * D_;
  hipMemsetAsync(kvz, 0, 65536, stream);
  k1_gemm<<<16 * 99, 256, 0, stream>>>(x, wqkv, w1, pq, pk, vv, hh);
  k2_kv<<<dim3(32, 8), 384, 0, stream>>>(pk, vv, kvz);
  k2b_o<<<S_ * D_ / 256, 256, 0, stream>>>(pq, kvz, o);
  k3_out<<<16 * 9, 256, 0, stream>>>(x, gw, o, wo, hh, w2, (u16*)d_out);
}
```

```cpp
#include <hip/hip_runtime.h>
#include <hip/hip_bf16.h>
#include <stdint.h>

typedef __attribute__((ext_vector_type(8))) short bf16x8;
typedef __attribute__((ext_vector_type(4))) short s16x4;
typedef __attribute__((ext_vector_type(4))) float f32x4;
typedef __attribute__((address_space(3))) s16x4* lds_s4p;
typedef unsigned short u16;

#define S_ 2048
#define D_ 576
#define DFF_ 4608
#define DH_ 18

__device__ __forceinline__ float bf2f(u16 v) { return __uint_as_float(((unsigned)v) << 16); }
__device__ __forceinline__ u16 f2bf(float f) {
  __hip_bfloat16 b = __float2bfloat16(f);
  return *reinterpret_cast<u16*>(&b);
}
__device__ __forceinline__ float rbf(float f) { return bf2f(f2bf(f)); }
__device__ __forceinline__ float phi_f(float t) {
  float r = t > 0.f ? rbf(t + 1.f) : rbf(__expf(t));
  return rbf(r + 1e-6f);
}

constexpr int A_TILE = 128 * 128;
template <int NT> struct Cfg {
  static constexpr int CH = 2 * NT;
  static constexpr int RB = NT * 32;
  static constexpr int B_TILE = 64 * RB;
  static constexpr int STAGE = A_TILE + B_TILE;
};
template <int NT> __device__ __forceinline__ int bswz(int k, int ch) {
  if constexpr (NT == 8) return ch ^ (((k & 3) << 2) | ((k >> 2) & 3));
  else { int c = ch + 8 * ((k >> 3) & 1); return c >= 18 ? c - 18 : c; }
}
template <int NT> __device__ __forceinline__ int bunswz(int k, int chp) {
  if constexpr (NT == 8) return chp ^ (((k & 3) << 2) | ((k >> 2) & 3));
  else { int c = chp - 8 * ((k >> 3) & 1); return c < 0 ? c + 18 : c; }
}

template <int NT>
__device__ __forceinline__ void gemm_main(const u16* __restrict__ A, int lda, const u16* __restrict__ B, int ldb, int kbeg,
                                          int nk, int brow, int bcol, int split, int bcol2, char* smem, f32x4 (&acc)[NT][2]) {
  using C = Cfg<NT>;
  const int tid = threadIdx.x, lane = tid & 63, wid = __builtin_amdgcn_readfirstlane(tid >> 6);
  const u16* asrc[4];
  for (int i = 0; i < 4; ++i) {
    int L = (wid * 4 + i) * 64 + lane, r = L >> 3, chp = L & 7, ch = chp ^ (r & 7);
    asrc[i] = A + (size_t)(brow + r) * lda + kbeg + ch * 8;
  }
  constexpr int NBI = (64 * C::CH) / 64;
  constexpr int BPW = (NBI + 3) / 4;
  const u16* bsrc[BPW];
  for (int i = 0; i < BPW; ++i) {
    int ii = wid + 4 * i; if (ii >= NBI) ii = wid;
    int L = ii * 64 + lane, k = L / C::CH, chp = L % C::CH, ch = bunswz<NT>(k, chp);
    int col = ch * 8;
    col = (split > 0 && col >= split) ? bcol2 + col - split : bcol + col;
    bsrc[i] = B + (size_t)(kbeg + k) * ldb + col;
  }
  auto stage = [&](int buf, int t) {
    char* sA = smem + buf * C::STAGE;
    char* sB = sA + A_TILE;
    for (int i = 0; i < 4; ++i)
      __builtin_amdgcn_global_load_lds((const unsigned*)(asrc[i] + t * 64), (unsigned*)(sA + (wid * 4 + i) * 1024), 16, 0, 0);
    for (int i = 0; i < BPW; ++i) {
      int ii = wid + 4 * i;
      if (ii < NBI)
        __builtin_amdgcn_global_load_lds((const unsigned*)(bsrc[i] + (size_t)t * 64 * ldb), (unsigned*)(sB + ii * 1024), 16, 0, 0);
    }
  };
  stage(0, 0);
  asm volatile("s_waitcnt vmcnt(0)" ::: "memory");
  __syncthreads();
  for (int t = 0; t < nk; ++t) {
    int buf = t & 1;
    if (t + 1 < nk) stage(buf ^ 1, t + 1);
    char* sA = smem + buf * C::STAGE;
    char* sB = sA + A_TILE;
#pragma unroll
    for (int ks = 0; ks < 2; ++ks) {
      bf16x8 af[2];
      for (int m = 0; m < 2; ++m) {
        int r = wid * 32 + m * 16 + (lane & 15), ch = ks * 4 + (lane >> 4);
        af[m] = *reinterpret_cast<const bf16x8*>(sA + r * 128 + ((ch ^ (r & 7)) << 4));
      }
#pragma unroll
      for (int n = 0; n < NT; ++n) {
        s16x4 tt[2];
        for (int q = 0; q < 2; ++q) {
          int k = 32 * ks + 8 * (lane >> 4) + 4 * q + ((lane & 15) >> 2);
          int ch = 2 * n + ((lane & 3) >> 1);
          tt[q] = __builtin_amdgcn_ds_read_tr16_b64_v4i16((lds_s4p)(sB + k * C::RB + (bswz<NT>(k, ch) << 4) + (lane & 1) * 8));
        }
        bf16x8 bfr = (bf16x8){tt[0][0], tt[0][1], tt[0][2], tt[0][3], tt[1][0], tt[1][1], tt[1][2], tt[1][3]};
        for (int m = 0; m < 2; ++m) acc[n][m] = __builtin_amdgcn_mfma_f32_16x16x32_bf16(bfr, af[m], acc[n][m], 0, 0, 0);
      }
    }
    asm volatile("s_waitcnt vmcnt(0)" ::: "memory");
    __syncthreads();
  }
}

__global__ __launch_bounds__(256, 2) void k1_gemm(const u16* __restrict__ x, const u16* __restrict__ wqkv, const u16* __restrict__ w1,
                                                  u16* __restrict__ pq, u16* __restrict__ hh, float* __restrict__ kvz) {
  extern __shared__ __attribute__((aligned(16))) char smem[];
  const int tid = threadIdx.x, lane = tid & 63, wid = tid >> 6;
  int b = blockIdx.x;
  if (b < 192) {
    int bm = b / 12, j = b % 12, brow = bm * 128;
    f32x4 acc[9][2] = {};
    bool isq = j < 4;
    if (isq) gemm_main<9>(x, D_, wqkv, 3 * D_, 0, 9, brow, j * 144, 0, 0, smem, acc);
    else gemm_main<9>(x, D_, wqkv, 3 * D_, 0, 9, brow, D_ + (j - 4) * 72, 72, 2 * D_ + (j - 4) * 72, smem, acc);
    constexpr int ST = 288 + 16;
    for (int n = 0; n < 9; ++n) for (int m = 0; m < 2; ++m) {
      int r = wid * 32 + m * 16 + (lane & 15), c = n * 16 + (lane >> 4) * 4;
      u16 o4[4];
      for (int jj = 0; jj < 4; ++jj) {
        float t = rbf(acc[n][m][jj]);
        o4[jj] = f2bf((isq || c + jj < 72) ? phi_f(t) : t);
      }
      *reinterpret_cast<uint2*>(smem + r * ST + c * 2) = *reinterpret_cast<uint2*>(o4);
    }
    __syncthreads();
    if (isq) {
      for (int i = tid; i < 128 * 18; i += 256) {
        int r = i / 18, ch = i % 18;
        *reinterpret_cast<uint4*>(pq + (size_t)(brow + r) * D_ + j * 144 + ch * 8) = *reinterpret_cast<uint4*>(smem + r * ST + ch * 16);
      }
    } else {
      for (int t = tid; t < 4 * 342; t += 256) {
        int hl = t / 342, e = t % 342;
        float a = 0.f;
        if (e < 324) {
          int d = e / DH_, ee = e % DH_;
          const char* pa = smem + (hl * DH_ + d) * 2; const char* pb = smem + (72 + hl * DH_ + ee) * 2;
          for (int s = 0; s < 128; ++s) a += bf2f(*(const u16*)(pa + s * ST)) * bf2f(*(const u16*)(pb + s * ST));
        } else {
          const char* pa = smem + (hl * DH_ + e - 324) * 2;
          for (int s = 0; s < 128; ++s) a += bf2f(*(const u16*)(pa + s * ST));
        }
        atomicAdd(&kvz[((j - 4) * 4 + hl) * 342 + e], a);
      }
    }
  } else {
    b -= 192;
    int bm = b / 36, bn = b % 36, brow = bm * 128, bcol = bn * 128;
    f32x4 acc[8][2] = {};
    gemm_main<8>(x, D_, w1, DFF_, 0, 9, brow, bcol, 0, 0, smem, acc);
    constexpr int ST = 256 + 16;
    for (int n = 0; n < 8; ++n) for (int m = 0; m < 2; ++m) {
      int r = wid * 32 + m * 16 + (lane & 15), c = n * 16 + (lane >> 4) * 4;
      u16 o4[4];
      for (int jj = 0; jj < 4; ++jj) o4[jj] = f2bf(fmaxf(acc[n][m][jj], 0.f));
      *reinterpret_cast<uint2*>(smem + r * ST + c * 2) = *reinterpret_cast<uint2*>(o4);
    }
    __syncthreads();
    for (int i = tid; i < 128 * 16; i += 256) {
      int r = i >> 4, ch = i & 15;
      *reinterpret_cast<uint4*>(hh + (size_t)(brow + r) * DFF_ + bcol + ch * 8) = *reinterpret_cast<uint4*>(smem + r * ST + ch * 16);
    }
  }
}

__global__ __launch_bounds__(256, 2) void k3_gemm(const u16* __restrict__ hh, const u16* __restrict__ w2, float* __restrict__ part) {
  extern __shared__ __attribute__((aligned(16))) char smem[];
  const int lane = threadIdx.x & 63, wid = threadIdx.x >> 6;
  int b = blockIdx.x, sk = b & 3, bn = (b >> 2) & 3, bm = b >> 4;
  f32x4 acc[9][2] = {};
  gemm_main<9>(hh, DFF_, w2, D_, sk * 1152, 18, bm * 128, bn * 144, 0, 0, smem, acc);
  float* p = part + (size_t)sk * S_ * D_;
  for (int n = 0; n < 9; ++n) for (int m = 0; m < 2; ++m) {
    int r = bm * 128 + wid * 32 + m * 16 + (lane & 15), c = bn * 144 + n * 16 + (lane >> 4) * 4;
    *reinterpret_cast<f32x4*>(p + (size_t)r * D_ + c) = acc[n][m];
  }
}

__global__ __launch_bounds__(256) void k4_out(const u16* __restrict__ x, const u16* __restrict__ gw, const u16* __restrict__ pq,
                                              const float* __restrict__ kvz, const u16* __restrict__ wo, const float* __restrict__ part,
                                              u16* __restrict__ out) {
  __shared__ float skv[32 * 342];
  __shared__ float so[16][D_ + 1];
  __shared__ float sg[16];
  const int tid = threadIdx.x, r0 = blockIdx.x * 16;
  for (int i = tid; i < 32 * 342; i += 256) skv[i] = rbf(kvz[i]);
  {
    int r = tid >> 4, l = tid & 15;
    float s = 0.f;
    for (int d = l; d < D_; d += 16) s += rbf(bf2f(x[(size_t)(r0 + r) * D_ + d]) * bf2f(gw[d]));
    for (int o = 8; o; o >>= 1) s += __shfl_xor(s, o);
    if (l == 0) sg[r] = rbf(1.f / (1.f + __expf(-rbf(s))));
  }
  __syncthreads();
  for (int i = tid; i < 16 * D_; i += 256) {
    int r = i / D_, c = i % D_, h = c / DH_, e = c % DH_;
    float num = 0.f, den = 0.f;
    for (int d = 0; d < DH_; ++d) {
      float q = bf2f(pq[(size_t)(r0 + r) * D_ + h * DH_ + d]);
      num += q * skv[h * 342 + d * DH_ + e];
      den += q * skv[h * 342 + 324 + d];
    }
    so[r][c] = rbf(rbf(num) / rbf(rbf(den) + 1e-6f));
  }
  __syncthreads();
  for (int c = tid; c < D_; c += 256) {
    float a[16];
    for (int r = 0; r < 16; ++r) a[r] = 0.f;
    for (int k = 0; k < D_; ++k) { float w = bf2f(wo[(size_t)k * D_ + c]); for (int r = 0; r < 16; ++r) a[r] += so[r][k] * w; }
    for (int r = 0; r < 16; ++r) {
      size_t idx = (size_t)(r0 + r) * D_ + c;
      float b = part[idx] + part[idx + (size_t)S_ * D_] + part[idx + 2 * (size_t)S_ * D_] + part[idx + 3 * (size_t)S_ * D_];
      float g = sg[r];
      out[idx] = f2bf(rbf(g * rbf(a[r])) + rbf(rbf(1.f - g) * rbf(b)));
    }
  }
}

extern "C" void kernel_launch(void* const* d_in, const int* in_sizes, int n_in, void* d_out, int out_size, void* d_ws,
                              size_t ws_size, hipStream_t stream) {
  const u16* x = (const u16*)d_in[0];
  const u16* wqkv = (const u16*)d_in[1];
  const u16* wo = (const u16*)d_in[2];
  const u16* w1 = (const u16*)d_in[4];
  const u16* w2 = (const u16*)d_in[5];
  const u16* gw = (const u16*)d_in[6];
  char* ws = (char*)d_ws;
  float* kvz = (float*)ws;
  u16* pq = (u16*)(ws + 65536);
  u16* hh = pq + S_ * D_;
  float* part = (float*)(hh + (size_t)S_ * DFF_);
  constexpr int SM = 2 * Cfg<9>::STAGE;
  hipFuncSetAttribute((const void*)k1_gemm, hipFuncAttributeMaxDynamicSharedMemorySize, SM);
  hipFuncSetAttribute((const void*)k3_gemm, hipFuncAttributeMaxDynamicSharedMemorySize, SM);
  hipMemsetAsync(kvz, 0, 65536, stream);
  k1_gemm<<<768, 256, SM, stream>>>(x, wqkv, w1, pq, hh, kvz);
  k3_gemm<<<256, 256, SM, stream>>>(hh, w2, part);
  k4_out<<<128, 256, 0, stream>>>(x, gw, pq, kvz, wo, part, (u16*)d_out);
}
```

```cpp
#include <hip/hip_runtime.h>
#include <hip/hip_bf16.h>
#include <stdint.h>

typedef __attribute__((ext_vector_type(8))) short bf16x8;
typedef __attribute__((ext_vector_type(4))) short s16x4;
typedef __attribute__((ext_vector_type(4))) float f32x4;
typedef __attribute__((address_space(3))) s16x4* lds_s4p;
typedef unsigned short u16;

#define S_ 2048
#define D_ 576
#define DFF_ 4608
#define DH_ 18

__device__ __forceinline__ float bf2f(u16 v) { return __uint_as_float(((unsigned)v) << 16); }
__device__ __forceinline__ u16 f2bf(float f) {
  __hip_bfloat16 b = __float2bfloat16(f);
  return *reinterpret_cast<u16*>(&b);
}
__device__ __forceinline__ float rbf(float f) { return bf2f(f2bf(f)); }
__device__ __forceinline__ float phi_f(float t) {
  float r = t > 0.f ? rbf(t + 1.f) : rbf(__expf(t));
  return rbf(r + 1e-6f);
}

constexpr int A_TILE = 128 * 128;
template <int NT> struct Cfg {
  static constexpr int CH = 2 * NT;
  static constexpr int RB = NT * 32;
  static constexpr int B_TILE = 64 * RB;
  static constexpr int STAGE = A_TILE + B_TILE;
};
template <int NT> __device__ __forceinline__ int bswz(int k, int ch) {
  if constexpr (NT == 8) return ch ^ (((k & 3) << 2) | ((k >> 2) & 3));
  else { int c = ch + 8 * ((k >> 3) & 1); return c >= 18 ? c - 18 : c; }
}
template <int NT> __device__ __forceinline__ int bunswz(int k, int chp) {
  if constexpr (NT == 8) return chp ^ (((k & 3) << 2) | ((k >> 2) & 3));
  else { int c = chp - 8 * ((k >> 3) & 1); return c < 0 ? c + 18 : c; }
}

template <int NT>
__device__ __forceinline__ void gemm_main(const u16* __restrict__ A, int lda, const u16* __restrict__ B, int ldb, int kbeg,
                                          int nk, int brow, int bcol, int split, int bcol2, char* smem, f32x4 (&acc)[NT][2]) {
  using C = Cfg<NT>;
  const int tid = threadIdx.x, lane = tid & 63, wid = __builtin_amdgcn_readfirstlane(tid >> 6);
  const u16* asrc[4];
  for (int i = 0; i < 4; ++i) {
    int L = (wid * 4 + i) * 64 + lane, r = L >> 3, chp = L & 7, ch = chp ^ (r & 7);
    asrc[i] = A + (size_t)(brow + r) * lda + kbeg + ch * 8;
  }
  constexpr int NBI = (64 * C::CH) / 64;
  constexpr int BPW = (NBI + 3) / 4;
  const u16* bsrc[BPW];
  for (int i = 0; i < BPW; ++i) {
    int ii = wid + 4 * i; if (ii >= NBI) ii = wid;
    int L = ii * 64 + lane, k = L / C::CH, chp = L % C::CH, ch = bunswz<NT>(k, chp);
    int col = ch * 8;
    col = (split > 0 && col >= split) ? bcol2 + col - split : bcol + col;
    bsrc[i] = B + (size_t)(kbeg + k) * ldb + col;
  }
  auto stage = [&](int buf, int t) {
    char* sA = smem + buf * C::STAGE;
    char* sB = sA + A_TILE;
    for (int i = 0; i < 4; ++i)
      __builtin_amdgcn_global_load_lds((const unsigned*)(asrc[i] + t * 64), (unsigned*)(sA + (wid * 4 + i) * 1024), 16, 0, 0);
    for (int i = 0; i < BPW; ++i) {
      int ii = wid + 4 * i;
      if (ii < NBI)
        __builtin_amdgcn_global_load_lds((const unsigned*)(bsrc[i] + (size_t)t * 64 * ldb), (unsigned*)(sB + ii * 1024), 16, 0, 0);
    }
  };
  stage(0, 0);
  asm volatile("s_waitcnt vmcnt(0)" ::: "memory");
  __syncthreads();
  for (int t = 0; t < nk; ++t) {
    int buf = t & 1;
    if (t + 1 < nk) stage(buf ^ 1, t + 1);
    char* sA = smem + buf * C::STAGE;
    char* sB = sA + A_TILE;
#pragma unroll
    for (int ks = 0; ks < 2; ++ks) {
      bf16x8 af[2];
      for (int m = 0; m < 2; ++m) {
        int r = wid * 32 + m * 16 + (lane & 15), ch = ks * 4 + (lane >> 4);
        af[m] = *reinterpret_cast<const bf16x8*>(sA + r * 128 + ((ch ^ (r & 7)) << 4));
      }
#pragma unroll
      for (int n = 0; n < NT; ++n) {
        s16x4 tt[2];
        for (int q = 0; q < 2; ++q) {
          int k = 32 * ks + 8 * (lane >> 4) + 4 * q + ((lane & 15) >> 2);
          int ch = 2 * n + ((lane & 3) >> 1);
          tt[q] = __builtin_amdgcn_ds_read_tr16_b64_v4i16((lds_s4p)(sB + k * C::RB + (bswz<NT>(k, ch) << 4) + (lane & 1) * 8));
        }
        bf16x8 bfr = (bf16x8){tt[0][0], tt[0][1], tt[0][2], tt[0][3], tt[1][0], tt[1][1], tt[1][2], tt[1][3]};
        for (int m = 0; m < 2; ++m) acc[n][m] = __builtin_amdgcn_mfma_f32_16x16x32_bf16(bfr, af[m], acc[n][m], 0, 0, 0);
      }
    }
    asm volatile("s_waitcnt vmcnt(0)" ::: "memory");
    __syncthreads();
  }
}

__global__ __launch_bounds__(256, 2) void k1_gemm(const u16* __restrict__ x, const u16* __restrict__ wqkv, const u16* __restrict__ w1,
                                                  u16* __restrict__ pq, u16* __restrict__ hh, float* __restrict__ kvz) {
  extern __shared__ __attribute__((aligned(16))) char smem[];
  const int tid = threadIdx.x, lane = tid & 63, wid = tid >> 6;
  int b = blockIdx.x;
  if (b < 192) {
    int bm = b / 12, j = b % 12, brow = bm * 128;
    f32x4 acc[9][2] = {};
    bool isq = j < 4;
    if (isq) gemm_main<9>(x, D_, wqkv, 3 * D_, 0, 9, brow, j * 144, 0, 0, smem, acc);
    else gemm_main<9>(x, D_, wqkv, 3 * D_, 0, 9, brow, D_ + (j - 4) * 72, 72, 2 * D_ + (j - 4) * 72, smem, acc);
    constexpr int ST = 288 + 16;
    for (int n = 0; n < 9; ++n) for (int m = 0; m < 2; ++m) {
      int r = wid * 32 + m * 16 + (lane & 15), c = n * 16 + (lane >> 4) * 4;
      u16 o4[4];
      for (int jj = 0; jj < 4; ++jj) {
        float t = rbf(acc[n][m][jj]);
        o4[jj] = f2bf((isq || c + jj < 72) ? phi_f(t) : t);
      }
      *reinterpret_cast<uint2*>(smem + r * ST + c * 2) = *reinterpret_cast<uint2*>(o4);
    }
    __syncthreads();
    if (isq) {
      for (int i = tid; i < 128 * 18; i += 256) {
        int r = i / 18, ch = i % 18;
        *reinterpret_cast<uint4*>(pq + (size_t)(brow + r) * D_ + j * 144 + ch * 8) = *reinterpret_cast<uint4*>(smem + r * ST + ch * 16);
      }
    } else {
      for (int t = tid; t < 4 * 342; t += 256) {
        int hl = t / 342, e = t % 342;
        float a = 0.f;
        if (e < 324) {
          int d = e / DH_, ee = e % DH_;
          const char* pa = smem + (hl * DH_ + d) * 2; const char* pb = smem + (72 + hl * DH_ + ee) * 2;
          for (int s = 0; s < 128; ++s) a += bf2f(*(const u16*)(pa + s * ST)) * bf2f(*(const u16*)(pb + s * ST));
        } else {
          const char* pa = smem + (hl * DH_ + e - 324) * 2;
          for (int s = 0; s < 128; ++s) a += bf2f(*(const u16*)(pa + s * ST));
        }
        atomicAdd(&kvz[((j - 4) * 4 + hl) * 342 + e], a);
      }
    }
  } else {
    b -= 192;
    int bm = b / 36, bn = b % 36, brow = bm * 128, bcol = bn * 128;
    f32x4 acc[8][2] = {};
    gemm_main<8>(x, D_, w1, DFF_, 0, 9, brow, bcol, 0, 0, smem, acc);
    constexpr int ST = 256 + 16;
    for (int n = 0; n < 8; ++n) for (int m = 0; m < 2; ++m) {
      int r = wid * 32 + m * 16 + (lane & 15), c = n * 16 + (lane >> 4) * 4;
      u16 o4[4];
      for (int jj = 0; jj < 4; ++jj) o4[jj] = f2bf(fmaxf(acc[n][m][jj], 0.f));
      *reinterpret_cast<uint2*>(smem + r * ST + c * 2) = *reinterpret_cast<uint2*>(o4);
    }
    __syncthreads();
    for (int i = tid; i < 128 * 16; i += 256) {
      int r = i >> 4, ch = i & 15;
      *reinterpret_cast<uint4*>(hh + (size_t)(brow + r) * DFF_ + bcol + ch * 8) = *reinterpret_cast<uint4*>(smem + r * ST + ch * 16);
    }
  }
}

__global__ __launch_bounds__(256, 2) void k3_gemm(const u16* __restrict__ hh, const u16* __restrict__ w2, float* __restrict__ part,
                                                  const u16* __restrict__ pq, const float* __restrict__ kvz, const u16* __restrict__ wo,
                                                  u16* __restrict__ oscr) {
  extern __shared__ __attribute__((aligned(16))) char smem[];
  const int tid = threadIdx.x, lane = tid & 63, wid = tid >> 6;
  int b = blockIdx.x, sk, bn, bm;
  f32x4 acc[9][2] = {};
  if (b < 256) {
    sk = b & 3; bn = (b >> 2) & 3; bm = b >> 4;
  } else {
    b -= 256; sk = 4; bn = b & 3; bm = b >> 2;
    float* skv = (float*)smem;
    for (int i = tid; i < 32 * 342; i += 256) skv[i] = rbf(kvz[i]);
    __syncthreads();
    u16* om = oscr + (size_t)b * 128 * D_;
    for (int i = tid; i < 128 * 32; i += 256) {
      int r = i >> 5, h = i & 31;
      float q[DH_];
      const u16* qp = pq + (size_t)(bm * 128 + r) * D_ + h * DH_;
      for (int d = 0; d < DH_; ++d) q[d] = bf2f(qp[d]);
      const float* kv = skv + h * 342;
      float den = 0.f;
      for (int d = 0; d < DH_; ++d) den += q[d] * kv[324 + d];
      den = rbf(rbf(den) + 1e-6f);
#pragma unroll 1
      for (int e = 0; e < DH_; ++e) {
        float num = 0.f;
        for (int d = 0; d < DH_; ++d) num += q[d] * kv[d * DH_ + e];
        om[(size_t)r * D_ + h * DH_ + e] = f2bf(rbf(num) / den);
      }
    }
    __threadfence();
    asm volatile("s_waitcnt vmcnt(0)" ::: "memory");
    __syncthreads();
  }
  {
    bool ff = sk < 4;
    const u16* Ap = ff ? hh + (size_t)bm * 128 * DFF_ : oscr + (size_t)(blockIdx.x - 256) * 128 * D_;
    gemm_main<9>(Ap, ff ? DFF_ : D_, ff ? w2 : wo, D_, ff ? sk * 1152 : 0, ff ? 18 : 9, 0, bn * 144, 0, 0, smem, acc);
  }
  float* p = part + (size_t)sk * S_ * D_;
  for (int n = 0; n < 9; ++n) for (int m = 0; m < 2; ++m) {
    int r = bm * 128 + wid * 32 + m * 16 + (lane & 15), c = bn * 144 + n * 16 + (lane >> 4) * 4;
    *reinterpret_cast<f32x4*>(p + (size_t)r * D_ + c) = acc[n][m];
  }
}

__global__ __launch_bounds__(256) void k4_out(const u16* __restrict__ x, const u16* __restrict__ gw, const float* __restrict__ part,
                                              u16* __restrict__ out) {
  const int lane = threadIdx.x & 63, r = blockIdx.x * 4 + (threadIdx.x >> 6);
  float s = 0.f;
  for (int d = lane; d < D_; d += 64) s += rbf(bf2f(x[(size_t)r * D_ + d]) * bf2f(gw[d]));
  for (int o = 32; o; o >>= 1) s += __shfl_xor(s, o);
  float g = rbf(1.f / (1.f + __expf(-rbf(s)))), g1 = rbf(1.f - g);
  const size_t SL = (size_t)S_ * D_;
  for (int c = lane * 4; c < D_; c += 256) {
    const float* p = part + (size_t)r * D_ + c;
    f32x4 b = *(const f32x4*)p + *(const f32x4*)(p + SL) + *(const f32x4*)(p + 2 * SL) + *(const f32x4*)(p + 3 * SL);
    f32x4 a = *(const f32x4*)(p + 4 * SL);
    u16 o4[4];
    for (int j = 0; j < 4; ++j) o4[j] = f2bf(rbf(g * rbf(a[j])) + rbf(g1 * rbf(b[j])));
    *reinterpret_cast<uint2*>(out + (size_t)r * D_ + c) = *reinterpret_cast<uint2*>(o4);
  }
}

extern "C" void kernel_launch(void* const* d_in, const int* in_sizes, int n_in, void* d_out, int out_size, void* d_ws,
                              size_t ws_size, hipStream_t stream) {
  const u16* x = (const u16*)d_in[0];
  const u16* wqkv = (const u16*)d_in[1];
  const u16* wo = (const u16*)d_in[2];
  const u16* w1 = (const u16*)d_in[4];
  const u16* w2 = (const u16*)d_in[5];
  const u16* gw = (const u16*)d_in[6];
  char* ws = (char*)d_ws;
  float* kvz = (float*)ws;
  u16* pq = (u16*)(ws + 65536);
  u16* hh = pq + S_ * D_;
  float* part = (float*)(hh + (size_t)S_ * DFF_);
  constexpr int SM = 2 * Cfg<9>::STAGE;
  hipFuncSetAttribute((const void*)k1_gemm, hipFuncAttributeMaxDynamicSharedMemorySize, SM);
  hipFuncSetAttribute((const void*)k3_gemm, hipFuncAttributeMaxDynamicSharedMemorySize, SM);
  hipMemsetAsync(kvz, 0, 65536, stream);
  k1_gemm<<<768, 256, SM, stream>>>(x, wqkv, w1, pq, hh, kvz);
  u16* oscr = (u16*)(part + 5 * (size_t)S_ * D_);
  k3_gemm<<<320, 256, SM, stream>>>(hh, w2, part, pq, kvz, wo, oscr);
  k4_out<<<512, 256, 0, stream>>>(x, gw, part, (u16*)d_out);
}
```

```cpp
#include <hip/hip_runtime.h>
#include <hip/hip_bf16.h>
#include <stdint.h>

typedef __attribute__((ext_vector_type(8))) short bf16x8;
typedef __attribute__((ext_vector_type(4))) short s16x4;
typedef __attribute__((ext_vector_type(4))) float f32x4;
typedef __attribute__((address_space(3))) s16x4* lds_s4p;
typedef unsigned short u16;

#define S_ 2048
#define D_ 576
#define DFF_ 4608
#define DH_ 18

__device__ __forceinline__ float bf2f(u16 v) { return __uint_as_float(((unsigned)v) << 16); }
__device__ __forceinline__ u16 f2bf(float f) {
  __hip_bfloat16 b = __float2bfloat16(f);
  return *reinterpret_cast<u16*>(&b);
}
__device__ __forceinline__ float rbf(float f) { return bf2f(f2bf(f)); }
__device__ __forceinline__ float phi_f(float t) {
  float r = t > 0.f ? rbf(t + 1.f) : rbf(__expf(t));
  return rbf(r + 1e-6f);
}

constexpr int A_TILE = 128 * 128;
template <int NT> struct Cfg {
  static constexpr int CH = 2 * NT;
  static constexpr int RB = NT * 32;
  static constexpr int B_TILE = 64 * RB;
  static constexpr int STAGE = A_TILE + B_TILE;
};
template <int NT> __device__ __forceinline__ int bswz(int k, int ch) {
  if constexpr (NT == 8) return ch ^ (((k & 3) << 2) | ((k >> 2) & 3));
  else { int c = ch + 8 * ((k >> 3) & 1); return c >= 18 ? c - 18 : c; }
}
template <int NT> __device__ __forceinline__ int bunswz(int k, int chp) {
  if constexpr (NT == 8) return chp ^ (((k & 3) << 2) | ((k >> 2) & 3));
  else { int c = chp - 8 * ((k >> 3) & 1); return c < 0 ? c + 18 : c; }
}

template <int NT>
__device__ __forceinline__ void gemm_main(const u16* __restrict__ A, int lda, const u16* __restrict__ B, int ldb, int kbeg,
                                          int nk, int brow, int bcol, int split, int bcol2, char* smem, f32x4 (&acc)[NT][2]) {
  using C = Cfg<NT>;
  const int tid = threadIdx.x, lane = tid & 63, wid = __builtin_amdgcn_readfirstlane(tid >> 6);
  const u16* asrc[4];
  for (int i = 0; i < 4; ++i) {
    int L = (wid * 4 + i) * 64 + lane, r = L >> 3, chp = L & 7, ch = chp ^ (r & 7);
    asrc[i] = A + (size_t)(brow + r) * lda + kbeg + ch * 8;
  }
  constexpr int NBI = (64 * C::CH) / 64;
  constexpr int BPW = (NBI + 3) / 4;
  const u16* bsrc[BPW];
  for (int i = 0; i < BPW; ++i) {
    int ii = wid + 4 * i; if (ii >= NBI) ii = wid;
    int L = ii * 64 + lane, k = L / C::CH, chp = L % C::CH, ch = bunswz<NT>(k, chp);
    int col = ch * 8;
    col = (split > 0 && col >= split) ? bcol2 + col - split : bcol + col;
    bsrc[i] = B + (size_t)(kbeg + k) * ldb + col;
  }
  auto stage = [&](int buf, int t) {
    char* sA = smem + buf * C::STAGE;
    char* sB = sA + A_TILE;
    for (int i = 0; i < 4; ++i)
      __builtin_amdgcn_global_load_lds((const unsigned*)(asrc[i] + t * 64), (unsigned*)(sA + (wid * 4 + i) * 1024), 16, 0, 0);
    for (int i = 0; i < BPW; ++i) {
      int ii = wid + 4 * i;
      if (ii < NBI)
        __builtin_amdgcn_global_load_lds((const unsigned*)(bsrc[i] + (size_t)t * 64 * ldb), (unsigned*)(sB + ii * 1024), 16, 0, 0);
    }
  };
  stage(0, 0);
  asm volatile("s_waitcnt vmcnt(0)" ::: "memory");
  __syncthreads();
  for (int t = 0; t < nk; ++t) {
    int buf = t & 1;
    if (t + 1 < nk) stage(buf ^ 1, t + 1);
    char* sA = smem + buf * C::STAGE;
    char* sB = sA + A_TILE;
#pragma unroll
    for (int ks = 0; ks < 2; ++ks) {
      bf16x8 af[2];
      for (int m = 0; m < 2; ++m) {
        int r = wid * 32 + m * 16 + (lane & 15), ch = ks * 4 + (lane >> 4);
        af[m] = *reinterpret_cast<const bf16x8*>(sA + r * 128 + ((ch ^ (r & 7)) << 4));
      }
#pragma unroll
      for (int n = 0; n < NT; ++n) {
        s16x4 tt[2];
        for (int q = 0; q < 2; ++q) {
          int k = 32 * ks + 8 * (lane >> 4) + 4 * q + ((lane & 15) >> 2);
          int ch = 2 * n + ((lane & 3) >> 1);
          tt[q] = __builtin_amdgcn_ds_read_tr16_b64_v4i16((lds_s4p)(sB + k * C::RB + (bswz<NT>(k, ch) << 4) + (lane & 1) * 8));
        }
        bf16x8 bfr = (bf16x8){tt[0][0], tt[0][1], tt[0][2], tt[0][3], tt[1][0], tt[1][1], tt[1][2], tt[1][3]};
        for (int m = 0; m < 2; ++m) acc[n][m] = __builtin_amdgcn_mfma_f32_16x16x32_bf16(bfr, af[m], acc[n][m], 0, 0, 0);
      }
    }
    asm volatile("s_waitcnt vmcnt(0)" ::: "memory");
    __syncthreads();
  }
}

__global__ __launch_bounds__(256, 2) void k1_gemm(const u16* __restrict__ x, const u16* __restrict__ wqkv, const u16* __restrict__ w1,
                                                  u16* __restrict__ pq, u16* __restrict__ hh, float* __restrict__ kvz) {
  extern __shared__ __attribute__((aligned(16))) char smem[];
  const int tid = threadIdx.x, lane = tid & 63, wid = tid >> 6;
  int b = blockIdx.x;
  if (b < 192) {
    int bm = b / 12, j = b % 12, brow = bm * 128;
    f32x4 acc[9][2] = {};
    bool isq = j < 4;
    if (isq) gemm_main<9>(x, D_, wqkv, 3 * D_, 0, 9, brow, j * 144, 0, 0, smem, acc);
    else gemm_main<9>(x, D_, wqkv, 3 * D_, 0, 9, brow, D_ + (j - 4) * 72, 72, 2 * D_ + (j - 4) * 72, smem, acc);
    constexpr int ST = 288 + 16;
    for (int n = 0; n < 9; ++n) for (int m = 0; m < 2; ++m) {
      int r = wid * 32 + m * 16 + (lane & 15), c = n * 16 + (lane >> 4) * 4;
      u16 o4[4];
      for (int jj = 0; jj < 4; ++jj) {
        float t = rbf(acc[n][m][jj]);
        o4[jj] = f2bf((isq || c + jj < 72) ? phi_f(t) : t);
      }
      *reinterpret_cast<uint2*>(smem + r * ST + c * 2) = *reinterpret_cast<uint2*>(o4);
    }
    __syncthreads();
    if (isq) {
      for (int i = tid; i < 128 * 18; i += 256) {
        int r = i / 18, ch = i % 18;
        *reinterpret_cast<uint4*>(pq + (size_t)(brow + r) * D_ + j * 144 + ch * 8) = *reinterpret_cast<uint4*>(smem + r * ST + ch * 16);
      }
    } else {
      for (int t = tid; t < 4 * 342; t += 256) {
        int hl = t / 342, e = t % 342;
        float a = 0.f;
        if (e < 324) {
          int d = e / DH_, ee = e % DH_;
          const char* pa = smem + (hl * DH_ + d) * 2; const char* pb = smem + (72 + hl * DH_ + ee) * 2;
          for (int s = 0; s < 128; ++s) a += bf2f(*(const u16*)(pa + s * ST)) * bf2f(*(const u16*)(pb + s * ST));
        } else {
          const char* pa = smem + (hl * DH_ + e - 324) * 2;
          for (int s = 0; s < 128; ++s) a += bf2f(*(const u16*)(pa + s * ST));
        }
        atomicAdd(&kvz[((j - 4) * 4 + hl) * 342 + e], a);
      }
    }
  } else {
    b -= 192;
    int bm = b / 36, bn = b % 36, brow = bm * 128, bcol = bn * 128;
    f32x4 acc[8][2] = {};
    gemm_main<8>(x, D_, w1, DFF_, 0, 9, brow, bcol, 0, 0, smem, acc);
    constexpr int ST = 256 + 16;
    for (int n = 0; n < 8; ++n) for (int m = 0; m < 2; ++m) {
      int r = wid * 32 + m * 16 + (lane & 15), c = n * 16 + (lane >> 4) * 4;
      u16 o4[4];
      for (int jj = 0; jj < 4; ++jj) o4[jj] = f2bf(fmaxf(acc[n][m][jj], 0.f));
      *reinterpret_cast<uint2*>(smem + r * ST + c * 2) = *reinterpret_cast<uint2*>(o4);
    }
    __syncthreads();
    for (int i = tid; i < 128 * 16; i += 256) {
      int r = i >> 4, ch = i & 15;
      *reinterpret_cast<uint4*>(hh + (size_t)(brow + r) * DFF_ + bcol + ch * 8) = *reinterpret_cast<uint4*>(smem + r * ST + ch * 16);
    }
  }
}

__global__ __launch_bounds__(256) void k2_o(const u16* __restrict__ pq, const float* __restrict__ kvz, u16* __restrict__ o) {
  __shared__ float skv[32 * 343];
  const int tid = threadIdx.x;
  for (int i = tid; i < 32 * 342; i += 256) skv[(i / 342) * 343 + i % 342] = rbf(kvz[i]);
  __syncthreads();
  int r = blockIdx.x * 8 + (tid >> 5), h = tid & 31;
  float q[DH_];
  const u16* qp = pq + (size_t)r * D_ + h * DH_;
  for (int d = 0; d < DH_; ++d) q[d] = bf2f(qp[d]);
  const float* kv = skv + h * 343;
  float den = 0.f;
  for (int d = 0; d < DH_; ++d) den += q[d] * kv[324 + d];
  den = rbf(rbf(den) + 1e-6f);
  for (int e = 0; e < DH_; ++e) {
    float num = 0.f;
    for (int d = 0; d < DH_; ++d) num += q[d] * kv[d * DH_ + e];
    o[(size_t)r * D_ + h * DH_ + e] = f2bf(rbf(num) / den);
  }
}

__global__ __launch_bounds__(256, 2) void k3_gemm(const u16* __restrict__ hh, const u16* __restrict__ w2, float* __restrict__ part,
                                                  const u16* __restrict__ pq, const float* __restrict__ kvz, const u16* __restrict__ wo,
                                                  u16* __restrict__ oscr) {
  extern __shared__ __attribute__((aligned(16))) char smem[];
  const int tid = threadIdx.x, lane = tid & 63, wid = tid >> 6;
  int b = blockIdx.x, sk, bn, bm;
  f32x4 acc[9][2] = {};
  if (b < 256) {
    sk = b & 3; bn = (b >> 2) & 3; bm = b >> 4;
  } else {
    b -= 256; sk = 4; bn = b & 3; bm = b >> 2;
  }
  {
    bool ff = sk < 4;
    const u16* Ap = ff ? hh + (size_t)bm * 128 * DFF_ : oscr + (size_t)bm * 128 * D_;
    gemm_main<9>(Ap, ff ? DFF_ : D_, ff ? w2 : wo, D_, ff ? sk * 1152 : 0, ff ? 18 : 9, 0, bn * 144, 0, 0, smem, acc);
  }
  float* p = part + (size_t)sk * S_ * D_;
  for (int n = 0; n < 9; ++n) for (int m = 0; m < 2; ++m) {
    int r = bm * 128 + wid * 32 + m * 16 + (lane & 15), c = bn * 144 + n * 16 + (lane >> 4) * 4;
    *reinterpret_cast<f32x4*>(p + (size_t)r * D_ + c) = acc[n][m];
  }
}

__global__ __launch_bounds__(256) void k4_out(const u16* __restrict__ x, const u16* __restrict__ gw, const float* __restrict__ part,
                                              u16* __restrict__ out) {
  const int lane = threadIdx.x & 63, r = blockIdx.x * 4 + (threadIdx.x >> 6);
  float s = 0.f;
  for (int d = lane; d < D_; d += 64) s += rbf(bf2f(x[(size_t)r * D_ + d]) * bf2f(gw[d]));
  for (int o = 32; o; o >>= 1) s += __shfl_xor(s, o);
  float g = rbf(1.f / (1.f + __expf(-rbf(s)))), g1 = rbf(1.f - g);
  const size_t SL = (size_t)S_ * D_;
  for (int c = lane * 4; c < D_; c += 256) {
    const float* p = part + (size_t)r * D_ + c;
    f32x4 b = *(const f32x4*)p + *(const f32x4*)(p + SL) + *(const f32x4*)(p + 2 * SL) + *(const f32x4*)(p + 3 * SL);
    f32x4 a = *(const f32x4*)(p + 4 * SL);
    u16 o4[4];
    for (int j = 0; j < 4; ++j) o4[j] = f2bf(rbf(g * rbf(a[j])) + rbf(g1 * rbf(b[j])));
    *reinterpret_cast<uint2*>(out + (size_t)r * D_ + c) = *reinterpret_cast<uint2*>(o4);
  }
}

extern "C" void kernel_launch(void* const* d_in, const int* in_sizes, int n_in, void* d_out, int out_size, void* d_ws,
                              size_t ws_size, hipStream_t stream) {
  const u16* x = (const u16*)d_in[0];
  const u16* wqkv = (const u16*)d_in[1];
  const u16* wo = (const u16*)d_in[2];
  const u16* w1 = (const u16*)d_in[4];
  const u16* w2 = (const u16*)d_in[5];
  const u16* gw = (const u16*)d_in[6];
  char* ws = (char*)d_ws;
  float* kvz = (float*)ws;
  u16* pq = (u16*)(ws + 65536);
  u16* hh = pq + S_ * D_;
  float* part = (float*)(hh + (size_t)S_ * DFF_);
  constexpr int SM = 2 * Cfg<9>::STAGE;
  hipFuncSetAttribute((const void*)k1_gemm, hipFuncAttributeMaxDynamicSharedMemorySize, SM);
  hipFuncSetAttribute((const void*)k3_gemm, hipFuncAttributeMaxDynamicSharedMemorySize, SM);
  hipMemsetAsync(kvz, 0, 65536, stream);
  k1_gemm<<<768, 256, SM, stream>>>(x, wqkv, w1, pq, hh, kvz);
  u16* oscr = (u16*)(part + 5 * (size_t)S_ * D_);
  k2_o<<<256, 256, 0, stream>>>(pq, kvz, oscr);
  k3_gemm<<<320, 256, SM, stream>>>(hh, w2, part, pq, kvz, wo, oscr);
  k4_out<<<512, 256, 0, stream>>>(x, gw, part, (u16*)d_out);
}
```
